```python
import torch

def _softmax(x, axis=-1):
    return torch.softmax(x, dim=axis)

def _gelu(x):
    return torch.nn.functional.gelu(x, approximate='tanh')

def _silu(x):
    return torch.nn.functional.silu(x)

def _sigmoid(x):
    return torch.sigmoid(x)

def _layernorm(x, axis=-1, eps=1e-05):
    m = x.mean(dim=axis, keepdim=True)
    v = x.var(dim=axis, keepdim=True, unbiased=False)
    return (x - m) / torch.sqrt(v + eps)

def _scatter_add(tgt, idx, src, axis=0):
    return tgt.clone().index_add_(axis, idx, src)

def _segment_sum(src, seg, n):
    out = torch.zeros((n,) + src.shape[1:], dtype=src.dtype)
    return out.index_add_(0, seg, src)

def _pad(x, pw):
    flat = tuple((v for pair in reversed(pw) for v in pair))
    return torch.nn.functional.pad(x, flat)
kernel_name = 'adaLN_cond__sliding_block_cached__glu_gated_bilinear__decode'

def setup_inputs():

    _w = lambda a: (lambda t: t.to(torch.bfloat16) if t.is_floating_point() else t)(torch.as_tensor(a))

    def _A_setup():
        B, Sq, D = (1, 1, 512)
        Hq, Hkv, Sk, Dh = (32, 8, 32768, 128)
        assert Hq % Hkv == 0, (Hq, Hkv)
        return {'x': _w((lambda *s: torch.randn(*s, dtype=torch.bfloat16) * 0.05)(B, Sq, D)), 'k_cache': _w((lambda *s: torch.randn(*s, dtype=torch.bfloat16) * 0.8)(B, Hkv, Sk, Dh)), 'v_cache': _w((lambda *s: torch.randn(*s, dtype=torch.bfloat16) * 3.2)(B, Hkv, Sk, Dh)), 'wq': _w((lambda *s: torch.randn(*s, dtype=torch.bfloat16) * 0.8)(D, Hq * Dh) * D ** (-0.5)), 'wo': _w((lambda *s: torch.randn(*s, dtype=torch.bfloat16) * 3.2)(Hq * Dh, D) * (Hq * Dh) ** (-0.5))}
    _A_in = _A_setup()

    def _B_setup():
        B, S, D, F = (1, 1, 512, 1792)
        return {'x': _w((lambda *s: torch.randn(*s, dtype=torch.bfloat16) * 0.05)(B, S, D)), 'w_gate': _w((lambda *s: torch.randn(*s, dtype=torch.bfloat16) * 3.2)(D, F) * D ** (-0.5)), 'w_up': _w((lambda *s: torch.randn(*s, dtype=torch.bfloat16) * 3.2)(D, F) * D ** (-0.5)), 'w_down': _w((lambda *s: torch.randn(*s, dtype=torch.bfloat16) * 3.2)(F, D) * F ** (-0.5))}
    _B_in = _B_setup()
    return {'A_x': _A_in['x'], 'A_k_cache': _A_in['k_cache'], 'A_v_cache': _A_in['v_cache'], 'A_wq': _A_in['wq'], 'A_wo': _A_in['wo'], 'B_x': _B_in['x'], 'B_w_gate': _B_in['w_gate'], 'B_w_up': _B_in['w_up'], 'B_w_down': _B_in['w_down'], 'c': _w((lambda *s: torch.randn(*s, dtype=torch.bfloat16) * 0.4)(1, 512) * 8.0), 'w_mod': _w((lambda *s: torch.randn(*s, dtype=torch.bfloat16) * 0.4)(512, 6 * 512) * 512 ** (-0.5) * 8.0)}

def reference(A_x, A_k_cache, A_v_cache, A_wq, A_wo, B_x, B_w_gate, B_w_up, B_w_down, c, w_mod):

    def _A_ref(x, k_cache, v_cache, wq, wo):
        B, Sq, D = x.shape
        Hq, Dh = (32, 128)
        Hkv, Sk = (k_cache.shape[1], k_cache.shape[2])
        W = min(4096, max(256, Sk // 4))
        rep = Hq // Hkv
        q = (lambda x, *d: x.permute(*d))((x @ wq).reshape(B, Sq, Hq, Dh), 0, 2, 1, 3)
        k = (lambda x, n, axis: torch.repeat_interleave(x, n, dim=axis))(k_cache[:, :, Sk - W:, :], rep, axis=1)
        v = (lambda x, n, axis: torch.repeat_interleave(x, n, dim=axis))(v_cache[:, :, Sk - W:, :], rep, axis=1)
        scores = torch.einsum('bhqd,bhkd->bhqk', q, k) * Dh ** (-0.5)
        p = _softmax(scores, axis=-1)
        o = torch.einsum('bhqk,bhkd->bhqd', p, v)
        return (lambda x, *d: x.permute(*d))(o, 0, 2, 1, 3).reshape(B, Sq, Hq * Dh) @ wo

    def _B_ref(x, w_gate, w_up, w_down):
        g = torch.einsum('bsd,df->bsf', x, w_gate)
        u = torch.einsum('bsd,df->bsf', x, w_up)
        h = g * u
        return torch.einsum('bsf,fd->bsd', h, w_down)
    x = A_x
    mod = (_silu(c) @ w_mod).reshape(c.shape[0], 6, 1, -1)
    sa, ca, ga, sb, cb, gb = (mod[:, 0].clone(), mod[:, 1].clone(), mod[:, 2].clone(), mod[:, 3].clone(), mod[:, 4].clone(), mod[:, 5].clone())
    h = _layernorm(x, axis=-1) * (1 + ca) + sa
    x = x + ga * _A_ref(h, A_k_cache, A_v_cache, A_wq, A_wo)
    h = _layernorm(x, axis=-1) * (1 + cb) + sb
    x = x + gb * _B_ref(h, B_w_gate, B_w_up, B_w_down)
    return x
```

```cpp
#include <hip/hip_runtime.h>
#include <hip/hip_bf16.h>
typedef unsigned short bf16;
__device__ __forceinline__ float bf2f(bf16 v){ return __builtin_bit_cast(float, (unsigned)v<<16); }
__device__ __forceinline__ float rbf(float f){ unsigned u=__builtin_bit_cast(unsigned,f); u=(u+0x7fffu+((u>>16)&1u))&0xffff0000u; return __builtin_bit_cast(float,u);}
constexpr int D=512, HQ=32, HKV=8, SK=32768, DH=128, W=4096, F=1792;
__global__ void k_mod(const bf16* c, const bf16* wm, float* mod){
  __shared__ float red[4][64]; __shared__ float s[D];
  for(int i=threadIdx.x;i<D;i+=256){ float v=bf2f(c[i]); s[i]=rbf(v/(1.f+__expf(-v))); }
  __syncthreads();
  int n=blockIdx.x*64+(threadIdx.x&63), kg=threadIdx.x>>6; float a=0;
  for(int k=kg*128;k<kg*128+128;k++) a+=s[k]*bf2f(wm[k*3072+n]);
  red[kg][threadIdx.x&63]=a; __syncthreads();
  if(kg==0) mod[n]=rbf(red[0][threadIdx.x]+red[1][threadIdx.x]+red[2][threadIdx.x]+red[3][threadIdx.x]);
}
__device__ void ln_mod(const float* xs, const float* sh, const float* sc, float* h){
  float m=0; for(int i=0;i<D;i++) m+=xs[i]; m/=D;
  float v=0; for(int i=0;i<D;i++){float d=xs[i]-m; v+=d*d;} v/=D;
  float r=1.f/sqrtf(v+1e-5f);
  for(int i=threadIdx.x;i<D;i+=256) h[i]=rbf((xs[i]-m)*r*(1.f+sc[i])+sh[i]);
}
__global__ void k_q(const bf16* x, const float* mod, const bf16* wq, float* q){
  __shared__ float red[4][64]; __shared__ float xs[D]; __shared__ float h[D];
  for(int i=threadIdx.x;i<D;i+=256) xs[i]=bf2f(x[i]);
  __syncthreads(); ln_mod(xs, mod, mod+D, h); __syncthreads();
  int n=blockIdx.x*64+(threadIdx.x&63), kg=threadIdx.x>>6; float a=0;
  for(int k=kg*128;k<kg*128+128;k++) a+=h[k]*bf2f(wq[k*4096+n]);
  red[kg][threadIdx.x&63]=a; __syncthreads();
  if(kg==0) q[n]=rbf(red[0][threadIdx.x]+red[1][threadIdx.x]+red[2][threadIdx.x]+red[3][threadIdx.x]);
}
__global__ void __launch_bounds__(128) k_attn(const float* q, const bf16* K, const bf16* V, float* po, float* pm, float* pl){
  int h=blockIdx.x>>5, s=blockIdx.x&31, g=h>>2, t=threadIdx.x;
  __shared__ float p[128]; __shared__ float qs[128];
  qs[t]=q[h*128+t]; __syncthreads();
  const bf16* kp=K+((size_t)g*SK+(SK-W)+s*128+t)*DH; float a=0;
  for(int d=0;d<128;d++) a+=qs[d]*bf2f(kp[d]);
  a*=0.08838834764831845f; p[t]=a; __syncthreads();
  float m=-1e30f; for(int i=0;i<128;i++) m=fmaxf(m,p[i]);
  __syncthreads(); p[t]=__expf(a-m); __syncthreads();
  float l=0; for(int i=0;i<128;i++) l+=p[i];
  const bf16* vp=V+((size_t)g*SK+(SK-W)+s*128)*DH+t; float o=0;
  for(int i=0;i<128;i++) o+=p[i]*bf2f(vp[i*DH]);
  po[(h*32+s)*128+t]=o; if(t==0){pm[h*32+s]=m; pl[h*32+s]=l;}
}
__global__ void k_comb(const float* po, const float* pm, const float* pl, float* o){
  int h=blockIdx.x, t=threadIdx.x; float M=-1e30f; for(int s=0;s<32;s++) M=fmaxf(M,pm[h*32+s]);
  float L=0,a=0; for(int s=0;s<32;s++){float e=__expf(pm[h*32+s]-M); L+=e*pl[h*32+s]; a+=e*po[(h*32+s)*128+t];}
  o[h*128+t]=rbf(a/L);
}
__global__ void k_wo(const bf16* x, const float* mod, const float* o, const bf16* wo, float* x1){
  __shared__ float red[16][64];
  int n=blockIdx.x*64+(threadIdx.x&63), kg=threadIdx.x>>6; float a=0;
  for(int k=kg*256;k<kg*256+256;k++) a+=o[k]*bf2f(wo[k*512+n]);
  red[kg][threadIdx.x&63]=a; __syncthreads();
  if(kg==0){ float s=0; for(int i=0;i<16;i++) s+=red[i][threadIdx.x]; x1[n]=rbf(bf2f(x[n])+rbf(mod[2*D+n]*rbf(s))); }
}
__global__ void k_gu(const float* x1, const float* mod, const bf16* wg, const bf16* wu, float* hh){
  __shared__ float red[2][4][64]; __shared__ float xs[D]; __shared__ float h[D];
  for(int i=threadIdx.x;i<D;i+=256) xs[i]=x1[i];
  __syncthreads(); ln_mod(xs, mod+3*D, mod+4*D, h); __syncthreads();
  int n=blockIdx.x*64+(threadIdx.x&63), kg=threadIdx.x>>6; float a=0,b=0;
  for(int k=kg*128;k<kg*128+128;k++){ a+=h[k]*bf2f(wg[k*F+n]); b+=h[k]*bf2f(wu[k*F+n]);}
  red[0][kg][threadIdx.x&63]=a; red[1][kg][threadIdx.x&63]=b; __syncthreads();
  if(kg==0){int t=threadIdx.x; float g=rbf(red[0][0][t]+red[0][1][t]+red[0][2][t]+red[0][3][t]), u=rbf(red[1][0][t]+red[1][1][t]+red[1][2][t]+red[1][3][t]); hh[n]=rbf(g*u);}
}
__global__ void k_down(const float* x1, const float* mod, const float* hh, const bf16* wd, bf16* out){
  __shared__ float red[16][64];
  int n=blockIdx.x*64+(threadIdx.x&63), kg=threadIdx.x>>6; float a=0;
  for(int k=kg*112;k<kg*112+112;k++) a+=hh[k]*bf2f(wd[k*512+n]);
  red[kg][threadIdx.x&63]=a; __syncthreads();
  if(kg==0){ float s=0; for(int i=0;i<16;i++) s+=red[i][threadIdx.x]; float r=rbf(x1[n]+rbf(mod[5*D+n]*rbf(s))); out[n]=(bf16)(__builtin_bit_cast(unsigned,r)>>16);}
}
extern "C" void kernel_launch(void* const* d_in, const int* in_sizes, int n_in, void* d_out, int out_size, void* d_ws, size_t ws_size, hipStream_t stream){
  const bf16* x=(const bf16*)d_in[0]; const bf16* K=(const bf16*)d_in[1]; const bf16* V=(const bf16*)d_in[2];
  const bf16* wq=(const bf16*)d_in[3]; const bf16* wo=(const bf16*)d_in[4];
  const bf16* wg=(const bf16*)d_in[6]; const bf16* wu=(const bf16*)d_in[7]; const bf16* wd=(const bf16*)d_in[8];
  const bf16* c=(const bf16*)d_in[9]; const bf16* wm=(const bf16*)d_in[10];
  float* ws=(float*)d_ws; float* mod=ws; float* q=mod+3072; float* po=q+4096; float* pm=po+32*32*128; float* pl=pm+1024; float* o=pl+1024; float* x1=o+4096; float* hh=x1+512;
  k_mod<<<48,256,0,stream>>>(c,wm,mod);
  k_q<<<64,256,0,stream>>>(x,mod,wq,q);
  k_attn<<<1024,128,0,stream>>>(q,K,V,po,pm,pl);
  k_comb<<<32,128,0,stream>>>(po,pm,pl,o);
  k_wo<<<8,1024,0,stream>>>(x,mod,o,wo,x1);
  k_gu<<<28,256,0,stream>>>(x1,mod,wg,wu,hh);
  k_down<<<8,1024,0,stream>>>(x1,mod,hh,wd,(bf16*)d_out);
}
```

```cpp
#include <hip/hip_runtime.h>
typedef unsigned u32;
typedef unsigned short u16;
typedef unsigned long long u64;
typedef u32 u32x4 __attribute__((ext_vector_type(4)));
typedef u32 u32x2 __attribute__((ext_vector_type(2)));
typedef float f32x4 __attribute__((ext_vector_type(4)));
typedef float f32x2 __attribute__((ext_vector_type(2)));
typedef __bf16 bf16x8 __attribute__((ext_vector_type(8)));
typedef __bf16 bf16x2 __attribute__((ext_vector_type(2)));
typedef short s16x4 __attribute__((ext_vector_type(4)));
#define DEV __device__ __forceinline__
#define FENCE() do { asm volatile("" ::: "memory"); __builtin_amdgcn_sched_barrier(0); } while (0)
#define BAR() do { asm volatile("s_waitcnt lgkmcnt(0)" ::: "memory"); __builtin_amdgcn_s_barrier(); asm volatile("" ::: "memory"); } while (0)

#define H_WAIT 2
#define Q_WAIT 1
#define Q_SLEEP 0
#define LATE_EARLY 1
#define WARM_KB 0
#define STR2(x) #x
#define STR(x) STR2(x)

constexpr u32 OFF_EP = 0, OFF_H1 = 0x1000, OFF_H2 = 0x10000, OFF_F3 = 0x20000, OFF_M3 = 0x28000, OFF_P = 0x30000,
              OFF_Y = 0x70000, OFF_F4 = 0x74000, OFF_ML = 0x78000, OFF_Z = 0x80000, WS_BYTES = 0xC0000;
constexpr u32 OOB = 0x40000000u;
constexpr int SPIN = 1 << 15;
constexpr u32 SENT = 0xFFFFFFFFu;

DEV float asf(u32 x) { return __builtin_bit_cast(float, x); }
DEV u32 asu(float x) { return __builtin_bit_cast(u32, x); }
DEV u32 pk(float a, float b) { bf16x2 v = __builtin_convertvector(f32x2{a, b}, bf16x2); return __builtin_bit_cast(u32, v); }
DEV float rbf(float a) { return asf(pk(a, a) & 0xFFFF0000u); }
DEV float lo16(u32 x) { return asf(x << 16); }
DEV float hi16(u32 x) { return asf(x & 0xFFFF0000u); }
DEV u32 clampS(u32 x) { return x < 0xFFFFFFFEu ? x : 0xFFFFFFFEu; }
DEV u32 umax(u32 a, u32 b) { return a > b ? a : b; }
DEV u32 umax4(u32x4 v) { return umax(umax(v.x, v.y), umax(v.z, v.w)); }
template <int C> DEV u32 dppu(u32 x) { return __builtin_amdgcn_update_dpp(0, x, C, 0xF, 0xF, true); }
template <int C> DEV float dppf(float x) { return asf(dppu<C>(asu(x))); }
constexpr int ROR4 = 0x124, ROR8 = 0x128, HMIR = 0x141, QX1 = 0xB1, QX2 = 0x4E;
DEV float red32(float x, float y) { auto r = __builtin_amdgcn_permlane32_swap(asu(x), asu(y), false, false); return asf(r[0]) + asf(r[1]); }
DEV float red16(float x, float y) { auto r = __builtin_amdgcn_permlane16_swap(asu(x), asu(y), false, false); return asf(r[0]) + asf(r[1]); }
DEV float max32(float x) { auto r = __builtin_amdgcn_permlane32_swap(asu(x), asu(x), false, false); return fmaxf(asf(r[0]), asf(r[1])); }
DEV float max16(float x) { auto r = __builtin_amdgcn_permlane16_swap(asu(x), asu(x), false, false); return fmaxf(asf(r[0]), asf(r[1])); }
DEV float tred4(f32x4 a) { return red16(red32(a.x, a.z), red32(a.y, a.w)); }
DEV float sumhi(float x) { x += dppf<ROR4>(x); x += dppf<ROR8>(x); x = red16(x, x); return red32(x, x); }
DEV float maxhi(float x) { x = fmaxf(x, dppf<ROR4>(x)); x = fmaxf(x, dppf<ROR8>(x)); return max32(max16(x)); }
DEV float wsum(float x) { x += dppf<QX1>(x); x += dppf<QX2>(x); return sumhi(x); }
DEV f32x4 mfma(u32x4 a, u32x4 b, f32x4 c) {
  return __builtin_amdgcn_mfma_f32_16x16x32_bf16(__builtin_bit_cast(bf16x8, a), __builtin_bit_cast(bf16x8, b), c, 0, 0, 0);
}
template <int I> DEV u32x4 selq(u32 x, u32x4 M) { u32 t = dppu<I * 0x55>(x); return u32x4{t & M.x, t & M.y, t & M.z, t & M.w}; }
template <int S> DEV u32x4 sel8(u32 x, u32 xs, u32x4 M) {
  u32 a = S < 4 ? dppu<(S & 3) * 0x55>(x) : dppu<((7 - S) & 3) * 0x55>(xs);
  u32 b = S < 4 ? dppu<((3 - S) & 3) * 0x55>(xs) : dppu<(S & 3) * 0x55>(x);
  return u32x4{a & M.x, a & M.y, b & M.z, b & M.w};
}
DEV f32x4 form32(u32 x, const u32x4* W, u32x4 M, bool b3) {
  u32 xs = dppu<HMIR>(x);
  f32x4 a0 = {0, 0, 0, 0}, a1 = {0, 0, 0, 0};
  a0 = mfma(sel8<0>(x, xs, M), W[0], a0); a1 = mfma(sel8<1>(x, xs, M), W[0], a1);
  a0 = mfma(sel8<2>(x, xs, M), W[1], a0); a1 = mfma(sel8<3>(x, xs, M), W[1], a1);
  a0 = mfma(sel8<4>(x, xs, M), W[2], a0); a1 = mfma(sel8<5>(x, xs, M), W[2], a1);
  a0 = mfma(sel8<6>(x, xs, M), W[3], a0); a1 = mfma(sel8<7>(x, xs, M), W[3], a1);
  return b3 ? a1 : a0;
}
DEV u32x4 ldnt(const void* p) { return __builtin_nontemporal_load((const u32x4*)p); }
DEV float silu(float v) { return v / (1.f + __expf(-v)); }
DEV u32x4 tagged(f32x4 v, u32 tag) {
  return u32x4{((asu(v.x) + 0x80u) & ~0xFFu) | (tag & 0xFF), ((asu(v.y) + 0x80u) & ~0xFFu) | ((tag >> 8) & 0xFF),
               ((asu(v.z) + 0x80u) & ~0xFFu) | ((tag >> 16) & 0xFF), ((asu(v.w) + 0x80u) & ~0xFFu) | (tag >> 24)};
}
DEV bool tagbad(u32x4 v, u32 tag) { return (__builtin_amdgcn_perm(v.y, v.x, 0x0c0c0400u) | __builtin_amdgcn_perm(v.w, v.z, 0x04000c0cu)) != tag; }
DEV f32x4 untag(u32x4 v) { return f32x4{asf(v.x & ~0xFFu), asf(v.y & ~0xFFu), asf(v.z & ~0xFFu), asf(v.w & ~0xFFu)}; }
DEV bool any(bool p) { return __builtin_amdgcn_ballot_w64(p) != 0; }
#define LD128(o) __builtin_amdgcn_raw_buffer_load_b128(rs, (o), 0, 16)
#define LD64(o) __builtin_amdgcn_raw_buffer_load_b64(rs, (o), 0, 16)
#define LD32(o) __builtin_amdgcn_raw_buffer_load_b32(rs, (o), 0, 16)
#define ST128(v, o) __builtin_amdgcn_raw_buffer_store_b128((v), rs, (o), 0, 16)
#define ST64(v, o) __builtin_amdgcn_raw_buffer_store_b64((v), rs, (o), 0, 16)
#define ST32(v, o) __builtin_amdgcn_raw_buffer_store_b32((v), rs, (o), 0, 16)
#define RETRY(bad, reload, ci) do { if (__builtin_expect(any(bad), 0)) { int n = 0; do { FENCE(); reload; ++n; } while (any(bad) && n < SPIN); CNT(ci, n); } FENCE(); } while (0)
#define STAMP(i)
#define CNT(i, n)

__global__ void __launch_bounds__(512, 2)
fused_block(const u16* x, const u16* Kc, const u16* Vc, const u16* wq, const u16* wo, const u16* wg, const u16* wu, const u16* wd,
            const u16* c, const u16* wm, u16* out, char* ws) {
  __shared__ __attribute__((aligned(16))) char kv[128 * 288];
  __shared__ __attribute__((aligned(16))) float red[16 * 80];
  __shared__ __attribute__((aligned(16))) u16 qs[4 * 136];
  __shared__ __attribute__((aligned(16))) float S[4 * 144];
  __shared__ __attribute__((aligned(16))) float wl[128];
  __shared__ __attribute__((aligned(16))) float st[16];
  __shared__ __attribute__((aligned(16))) float x1s[512];
  __shared__ __attribute__((aligned(16))) float gbs[512];
  STAMP(0);
  asm volatile("" ::"s"(x), "s"(wd), "s"(c), "s"(ws));
  const int b = blockIdx.x, g = b & 7, j = b >> 3;
  const int l = threadIdx.x & 63, w = __builtin_amdgcn_readfirstlane(threadIdx.x >> 6);
  const int kg = l >> 4, b5 = l >> 5, b4 = kg & 1, b3 = (l >> 3) & 1, b2 = (l >> 2) & 1;
  const auto rs = __builtin_amdgcn_make_buffer_rsrc(ws, 0, WS_BYTES, 0x00020000);
  const int ks = j & 3, cq = j >> 2;
  const int jj = j - 16, mi = 14 * g + jj;
  const int half = j & 1, cg = j >> 1;
  u32x4 M;
  { u32 mv = (l & 1) ? 0xFFFF0000u : 0xFFFFu; int q = (l & 7) >> 1; M = u32x4{q == 0 ? mv : 0, q == 1 ? mv : 0, q == 2 ? mv : 0, q == 3 ? mv : 0}; }
  u32 warm = 0;
  u32 ep = LD32(OFF_EP);
  u32 cL = c[(w == 0 ? 64 * g : 256 * ((jj >> 3) & 1) + 64 * (w & 3)) + l];
  const int rowb = 128 * ks + 32 * (w >> 1) + 4 * (w & 1) + kg;
  const int col2 = 64 * w + 8 * (l & 7) + 4 * b4 + 2 * b5 + b3;
  const u16* pq = wq + (rowb + 16 * b3) * 4096 + 512 * g + 64 * cq + 8 * (l & 7);
  u32x4 xf, Wq[2]; u16 xr, xc;
#define FIRST_LOADS() do { FENCE(); xf = *(const u32x4*)(x + 8 * l); xr = x[rowb + 8 * (l & 3)]; xc = x[col2]; Wq[0] = ldnt(pq); Wq[1] = ldnt(pq + 8 * 4096); FENCE(); } while (0)
  if (w == 0) {
    u32x4 WA[4];
    const u32x4 s4 = {SENT, SENT, SENT, SENT};
    ST128(s4, OFF_P + ((g * 8 + (l >> 3)) * 32 + j) * 128 + (l & 7) * 16);
    u32 o2 = j < 16 ? OFF_Y + (cg * 16 + 2 * g + half) * 128 : OFF_ML + ((jj >> 3) * 2048 + 32 * (8 * g + (jj & 7))) * 4;
    o2 = l < 8 ? o2 + l * 16 : l < 10 ? OFF_M3 + (g * 32 + j) * 128 + (l & 1) * 16 : OOB;
    ST128(s4, o2);
    ST128(s4, (j >= 16 && j < 30 ? OFF_Z + mi * 128 : OOB) + (l >> 2) * 112 * 128 + (l & 3) * 16);
    const u16* p = wm + (64 * g + 16 * kg + 8 * b2 + b3) * 3072 + 32 * j + 8 * (l & 3);
#pragma unroll
    for (int i = 0; i < 4; i++) WA[i] = ldnt(p + 2 * i * 3072);
    FIRST_LOADS();
    asm volatile("s_waitcnt vmcnt(5)" ::: "memory");
    const u32 tag = ep + 0x01010101u;
    float s = silu(lo16(cL));
    f32x4 a = form32(pk(s, s), WA, M, b3), r;
#pragma unroll
    for (int i = 0; i < 4; i++) {
      float t = a[i] + dppf<ROR8>(a[i]);
      auto sw = __builtin_amdgcn_permlane32_swap(asu(t), asu(t), false, false);
      r[i] = t + dppf<ROR4>(asf(sw[1]));
    }
    if ((l & 0x2C) == 0) ST128(tagged(r, tag), OFF_H1 + (j * 8 + g) * 128 + b4 * 64 + (l & 3) * 16);
    STAMP(1);
  } else FIRST_LOADS();
  FENCE();
  const size_t kvo = ((size_t)g * 32768 + 28672 + 128 * j + 16 * w) * 128 + 8 * l;
  u32x4 Kr[4], Vr[4];
#pragma unroll
  for (int i = 0; i < 4; i++) Kr[i] = ldnt(Kc + kvo + 512 * i);
  FENCE();
  const u32 tag = __builtin_amdgcn_readfirstlane(ep) + 0x01010101u;
  float ln;
  { float v[8] = {lo16(xf.x), hi16(xf.x), lo16(xf.y), hi16(xf.y), lo16(xf.z), hi16(xf.z), lo16(xf.w), hi16(xf.w)};
    float s = 0;
#pragma unroll
    for (int i = 0; i < 8; i++) s += v[i];
    float mean = wsum(s) * (1.f / 512), q = 0;
#pragma unroll
    for (int i = 0; i < 8; i++) q += (v[i] - mean) * (v[i] - mean);
    float var = wsum(q) * (1.f / 512);
    float sd = rbf(sqrtf(rbf(rbf(var) + 1e-5f)));
    ln = rbf(rbf(lo16(xr) - rbf(mean)) / sd);
  }
  asm volatile("s_waitcnt vmcnt(" STR(H_WAIT) ")" ::: "memory");
  FENCE();
  STAMP(2);
  const u32 oH1 = OFF_H1 + ((b2 * 16 + 4 * ks + (w >> 1)) * 8 + (l >> 3)) * 128 + (w & 1) * 64 + (l & 3) * 16;
  u32x4 hv = LD128(oH1);
  FENCE();
#pragma unroll
  for (int i = 0; i < 4; i++) Vr[i] = ldnt(Vc + kvo + 512 * i);
  FENCE();
  char* kvw = kv + (16 * w + kg) * 288 + (l & 15) * 16;
#pragma unroll
  for (int i = 0; i < 4; i++) *(u32x4*)(kvw + 4 * i * 288) = Kr[i];
  u32x4 Kf[4];
#pragma unroll
  for (int i = 0; i < 4; i++) Kf[i] = *(const u32x4*)(kv + (16 * w + (l & 15)) * 288 + (4 * i + kg) * 16);
  FENCE();
  RETRY(tagbad(hv, tag), hv = LD128(oH1), 12);
  STAMP(3);
  {
    float v = tred4(untag(hv)); v += dppf<ROR8>(v); v = rbf(v);
    float o = dppf<ROR4>(v), sa = b2 ? o : v, ca = b2 ? v : o;
    float h = rbf(ln * rbf(1.f + ca)) + sa;
    u32 hd = pk(h, h);
    hd = __builtin_amdgcn_update_dpp(hd, hd, 0xEE, 0xF, 0xC, false);
    f32x4 a = {0, 0, 0, 0};
    a = mfma(selq<0>(hd, M), Wq[0], a); a = mfma(selq<1>(hd, M), Wq[1], a);
    if (b5 == b3) *(f32x4*)(red + (2 * w + b3) * 80 + 8 * (l & 7) + 4 * b4) = a;
  }
  BAR();
  if (w == 0) {
    const float* p = red + ((l >> 2) & 3) * 80 + 4 * (4 * kg + (l & 3));
    f32x4 a = (*(const f32x4*)p + *(const f32x4*)(p + 320)) + (*(const f32x4*)(p + 640) + *(const f32x4*)(p + 960));
#pragma unroll
    for (int i = 0; i < 4; i++) { a[i] += dppf<ROR8>(a[i]); a[i] += dppf<ROR4>(a[i]); }
    if ((l & 0xC) == 0) ST128(tagged(a, tag), OFF_H2 + (g * 32 + j) * 256 + (4 * kg + (l & 3)) * 16);
    STAMP(4);
  }
  const u16 *pA = x, *pB = x; int sA = 0, sB = 0;
  if (j < 16) { pA = wo + (512 * g + 64 * half + 8 * w + (l >> 3)) * 512 + 64 * cg + 8 * (l & 7); sA = 128 * 512; }
  else {
    if (w >= 4) { pA = wm + (256 * (jj >> 3) + 64 * (w - 4) + 16 * kg + 8 * b2 + b3) * 3072 + 1024 + 32 * (8 * g + (jj & 7)) + 8 * (l & 3); sA = 2 * 3072; }
    if (j < 30) { pB = ((l & 2) ? wu : wg) + (64 * w + 8 * b3 + 4 * b4 + 2 * b5 + b2) * 1792 + 16 * mi + 8 * (l & 1); sB = 16 * 1792; }
  }
  asm volatile("s_waitcnt vmcnt(" STR(Q_WAIT) ")" ::: "memory");
  FENCE();
  STAMP(5);
  const u32 oH2 = OFF_H2 + g * 8192 + w * 1024 + l * 16;
  u32x4 qv = LD128(oH2);
  FENCE();
  u32x4 LA[4], LB[4];
#pragma unroll
  for (int i = 0; i < 4; i++) LA[i] = ldnt(pA + i * sA);
#pragma unroll
  for (int i = 0; i < 4; i++) LB[i] = ldnt(pB + i * sB);
  FENCE();
#pragma unroll
  for (int i = 0; i < 4; i++) *(u32x4*)(kvw + 4 * i * 288) = Vr[i];
  FENCE();
  RETRY(tagbad(qv, tag), qv = LD128(oH2), 13);
  { float q = tred4(untag(qv)); qs[(w >> 1) * 136 + (w & 1) * 64 + 4 * (l & 15) + kg] = (u16)pk(q, q); }
  BAR();
  STAMP(6);
  if (w == 1 && l == 0) ST32(tag, OFF_F3 + (g * 32 + j) * 128);
  u32x4 Vf[4];
  {
    f32x4 a = {0, 0, 0, 0};
#pragma unroll
    for (int i = 0; i < 4; i++) a = mfma(Kf[i], *(const u32x4*)(qs + (l & 3) * 136 + 32 * i + 8 * kg), a);
#pragma unroll
    for (int i = 0; i < 4; i++) {
      typedef __attribute__((address_space(3))) s16x4* lp;
      char* p = kv + (32 * i + 4 * kg + ((l >> 2) & 3)) * 288 + 32 * w + 8 * (l & 3);
      u32x2 t0 = __builtin_bit_cast(u32x2, __builtin_amdgcn_ds_read_tr16_b64_v4i16((lp)p));
      u32x2 t1 = __builtin_bit_cast(u32x2, __builtin_amdgcn_ds_read_tr16_b64_v4i16((lp)(p + 16 * 288)));
      Vf[i] = u32x4{t0.x, t0.y, t1.x, t1.y};
    }
#pragma unroll
    for (int i = 0; i < 4; i++) a[i] = rbf(rbf(a[i]) * 0.08838834764831845f);
    if ((l & 0xC) == 0) *(f32x4*)(S + (l & 3) * 144 + 16 * w + 4 * kg) = a;
  }
  BAR();
  {
    const float* p = S + (l & 3) * 144 + 32 * ((l >> 2) & 3) + 4 * kg;
    f32x4 s0 = *(const f32x4*)p, s1 = *(const f32x4*)(p + 16);
    float m = maxhi(fmaxf(fmaxf(fmaxf(s0.x, s0.y), fmaxf(s0.z, s0.w)), fmaxf(fmaxf(s1.x, s1.y), fmaxf(s1.z, s1.w))));
#pragma unroll
    for (int i = 0; i < 4; i++) { s0[i] = __builtin_amdgcn_exp2f((s0[i] - m) * 1.4426950408889634f); s1[i] = __builtin_amdgcn_exp2f((s1[i] - m) * 1.4426950408889634f); }
    u32x4 P = {pk(s0.x, s0.y), pk(s0.z, s0.w), pk(s1.x, s1.y), pk(s1.z, s1.w)};
    const f32x4 z = {0, 0, 0, 0};
    f32x4 d0 = mfma(Vf[0], P, z), d1 = mfma(Vf[1], P, z), d2 = mfma(Vf[2], P, z), d3 = mfma(Vf[3], P, z);
    if (w == 7) {
      float ls = sumhi(((s0.x + s0.y) + (s0.z + s0.w)) + ((s1.x + s1.y) + (s1.z + s1.w)));
      if (l < 4) ST64((u32x2{clampS(asu(m)), clampS(asu(ls))}), OFF_M3 + (g * 32 + j) * 128 + l * 8);
    }
    f32x4 d = b3 ? (b2 ? d3 : d2) : (b2 ? d1 : d0);
#pragma unroll
    for (int i = 0; i < 4; i++) { d[i] += dppf<ROR8>(d[i]); d[i] += dppf<ROR4>(d[i]); }
    u32 o0 = clampS(pk(d.x, d.y)), o1 = clampS(pk(d.z, d.w));
    auto r0 = __builtin_amdgcn_permlane16_swap(o0, o0, false, false), r1 = __builtin_amdgcn_permlane16_swap(o1, o1, false, false);
    if ((l & 0x1C) == 0) ST128((u32x4{r0[0], r1[0], r0[1], r1[1]}), OFF_P + ((g * 8 + w) * 32 + j) * 128 + b5 * 64 + (l & 3) * 16);
  }
  STAMP(7);
  FENCE();
  if (j < 16) {
    if (w == 0) { int n = 0; u32 f; do { FENCE(); f = LD32(OFF_F3 + (g * 32 + (l & 31)) * 128); } while (any(f != tag) && ++n < SPIN); }
    BAR();
    STAMP(8);
    u32x2 ml0, ml1;
    const u32 oM = OFF_M3 + (g * 32 + (l >> 2)) * 128 + (l & 3) * 8;
    if (w == 0) { ml0 = LD64(oM); ml1 = LD64(oM + 2048); }
    FENCE();
    const u32 oP = OFF_P + ((g * 8 + 4 * half + (w >> 1)) * 32 + (l >> 2)) * 128 + (w & 1) * 64 + (l & 3) * 16;
    u32x4 p0 = LD128(oP), p1 = LD128(oP + 2048);
    FENCE();
    if (w == 0) {
      RETRY(umax(umax(ml0.x, ml0.y), umax(ml1.x, ml1.y)) == SENT, (ml0 = LD64(oM), ml1 = LD64(oM + 2048)), 14);
      if (cg == 0 && l == 0) ST32(tag, OFF_F4 + (2 * g + half) * 128);
      float Mx = maxhi(fmaxf(asf(ml0.x), asf(ml1.x)));
      float e0 = __builtin_amdgcn_exp2f((asf(ml0.x) - Mx) * 1.4426950408889634f), e1 = __builtin_amdgcn_exp2f((asf(ml1.x) - Mx) * 1.4426950408889634f);
      float r = __builtin_amdgcn_rcpf(sumhi(e0 * asf(ml0.y) + e1 * asf(ml1.y)));
      *(f32x2*)(wl + 2 * l) = f32x2{e0 * r, e1 * r};
    }
    RETRY(umax(umax4(p0), umax4(p1)) == SENT, (p0 = LD128(oP), p1 = LD128(oP + 2048)), 15);
    BAR();
    STAMP(9);
    {
      f32x2 wv = *(const f32x2*)(wl + 2 * l);
      float a[8];
#pragma unroll
      for (int i = 0; i < 4; i++) { a[2 * i] = wv.x * lo16(p0[i]) + wv.y * lo16(p1[i]); a[2 * i + 1] = wv.x * hi16(p0[i]) + wv.y * hi16(p1[i]); }
      float t0 = red16(red32(a[0], a[4]), red32(a[2], a[6])), t1 = red16(red32(a[1], a[5]), red32(a[3], a[7]));
      t0 += dppf<ROR8>(t0); t1 += dppf<ROR8>(t1); t0 += dppf<ROR4>(t0); t1 += dppf<ROR4>(t1);
      float o = b3 ? t1 : t0;
      u32 od = pk(o, o);
      const f32x4 z = {0, 0, 0, 0};
      f32x4 a0 = mfma(selq<0>(od, M), LA[0], z), a1 = mfma(selq<1>(od, M), LA[1], z);
      a0 = mfma(selq<2>(od, M), LA[2], a0); a1 = mfma(selq<3>(od, M), LA[3], a1);
      if (b5 == b3) *(f32x4*)(red + (2 * w + b3) * 80 + 8 * (l & 7) + 4 * b4) = a0 + a1;
    }
    BAR();
    if (w == 0) {
      const float* p = red + ((l >> 2) & 3) * 80 + 4 * (8 * b5 + 2 * (l & 3) + b4);
      f32x4 a = (*(const f32x4*)p + *(const f32x4*)(p + 320)) + (*(const f32x4*)(p + 640) + *(const f32x4*)(p + 960));
#pragma unroll
      for (int i = 0; i < 4; i++) { a[i] += dppf<ROR8>(a[i]); a[i] += dppf<ROR4>(a[i]); }
      u32 o0 = clampS(pk(a.x, a.y)), o1 = clampS(pk(a.z, a.w));
      auto r0 = __builtin_amdgcn_permlane16_swap(o0, o0, false, false), r1 = __builtin_amdgcn_permlane16_swap(o1, o1, false, false);
      if ((l & 0x1C) == 0) ST128((u32x4{r0[0], r1[0], r0[1], r1[1]}), OFF_Y + (cg * 16 + 2 * g + half) * 128 + b5 * 64 + (l & 3) * 16);
    }
    STAMP(10);
  } else {
    u32x4 LC[4];
    { const u16* p = x; int s = 0;
      if (j < 30 && w < 4) { p = wd + (16 * mi + kg) * 512 + 128 * w + 8 * (l & 15); s = 4 * 512; }
#pragma unroll
      for (int i = 0; i < 4; i++) LC[i] = ldnt(p + i * s); }
    FENCE();
    if (w >= 4) {
      float s = silu(lo16(cL));
      f32x4 a = form32(pk(s, s), LA, M, b3);
      if (b5 == b2) *(f32x4*)(red + (4 * (w - 4) + ((l >> 2) & 3)) * 36 + 8 * (l & 3) + 4 * b4) = a;
    }
    BAR();
    if (w == 4) {
      const float* p = red + (l >> 3) * 36 + 4 * (l & 7);
      float v = tred4(*(const f32x4*)p + *(const f32x4*)(p + 288)); v += dppf<ROR8>(v);
      if (!b3) ST32(clampS(asu(v)), OFF_ML + ((jj >> 3) * 2048 + 32 * (8 * g + (jj & 7)) + 4 * (l & 7) + kg) * 4);
    }
    asm volatile("s_waitcnt vmcnt(0)" ::: "memory");
    FENCE();
    STAMP(8);
    float ga, sb, cb;
    { const u32 o0 = OFF_ML + (64 * w + l) * 4, o1 = o0 + (j >= 30 ? 6144 : 2048), o2 = o0 + 4096;
      u32 m0, m1, m2, m3, m4, m5; int n = 0;
      for (;;) {
        FENCE();
        m0 = LD32(o0); m1 = LD32(o0 + 8192); m2 = LD32(o1); m3 = LD32(o1 + 8192); m4 = LD32(o2); m5 = LD32(o2 + 8192);
        if (!any(umax(umax(umax(m0, m1), umax(m2, m3)), umax(m4, m5)) == SENT) || ++n >= SPIN) break;
        __builtin_amdgcn_s_sleep(4);
      }
      CNT(14, n);
      const int bp = 4 * (col2 & 63);
      ga = asf(__builtin_amdgcn_ds_bpermute(bp, asu(rbf(asf(m0) + asf(m1)))));
      sb = asf(__builtin_amdgcn_ds_bpermute(bp, asu(rbf(asf(m2) + asf(m3)))));
      cb = asf(__builtin_amdgcn_ds_bpermute(bp, asu(rbf(1.f + rbf(asf(m4) + asf(m5))))));
    }
    if (w == 0) { int n = 0; u32 f; do { FENCE(); f = LD32(OFF_F4 + (l & 15) * 128); } while (any(f != tag) && ++n < SPIN); }
    BAR();
    STAMP(9);
    const u32 oY = OFF_Y + w * 2048 + l * 16;
    u32x4 y0, y1;
    { int n = 0; do { FENCE(); y0 = LD128(oY); y1 = LD128(oY + 1024); } while (any(umax(umax4(y0), umax4(y1)) == SENT) && ++n < SPIN); CNT(15, n); }
    STAMP(10);
    const u32x4 ONE = {M.x & 0x3F803F80u, M.y & 0x3F803F80u, M.z & 0x3F803F80u, M.w & 0x3F803F80u};
    const f32x4 z = {0, 0, 0, 0};
    float x1;
    { f32x4 a = mfma(ONE, y1, mfma(ONE, y0, z));
      float v0 = b5 ? a.z : a.x, v1 = b5 ? a.w : a.y;
      float y = (b3 ? v1 : v0) + dppf<ROR8>(b3 ? v0 : v1);
      x1 = rbf(lo16(xc) + rbf(ga * rbf(y))); }
    if (j < 30) {
      float cw = asf(__builtin_amdgcn_readfirstlane(asu(x1))), d = x1 - cw;
      float s1 = wsum(d), s2 = wsum(d * d);
      if (l == 0) *(f32x2*)(st + 2 * w) = f32x2{cw + s1 * (1.f / 64), s2 - s1 * s1 * (1.f / 64)};
      BAR();
      f32x2 sv = *(const f32x2*)(st + 2 * (l & 7));
      float mean = sv.x; mean += dppf<QX1>(mean); mean += dppf<QX2>(mean); mean += dppf<HMIR>(mean); mean *= 0.125f;
      float dm = sv.x - mean, t = sv.y + 64.f * dm * dm; t += dppf<QX1>(t); t += dppf<QX2>(t); t += dppf<HMIR>(t);
      float sd = rbf(__builtin_amdgcn_sqrtf(rbf(rbf(fmaxf(t, 0.f) * (1.f / 512)) + 1e-5f)));
      float h = rbf(rbf(rbf(x1 - rbf(mean)) * __builtin_amdgcn_rcpf(sd)) * cb) + sb;
      f32x4 a = form32(pk(h, h), LB, M, b3);
      if (b5 == b2) *(f32x4*)(red + (4 * w + ((l >> 2) & 3)) * 36 + 8 * (l & 3) + 4 * b4) = a;
      BAR();
      if (w < 4) {
        const float* p = red + (l >> 3) * 36 + 4 * (l & 7);
        float v = tred4((*(const f32x4*)p + *(const f32x4*)(p + 288)) + (*(const f32x4*)(p + 576) + *(const f32x4*)(p + 864)));
        v += dppf<ROR8>(v); v = rbf(v); v *= dppf<ROR4>(v);
        u32 hd = pk(v, v);
        f32x4 a0 = mfma(selq<0>(hd, M), LC[0], z), a1 = mfma(selq<1>(hd, M), LC[1], z);
        a0 = mfma(selq<2>(hd, M), LC[2], a0); a1 = mfma(selq<3>(hd, M), LC[3], a1);
        a0 += a1;
        u32 o0 = clampS(pk(a0.x, a0.y)), o1 = clampS(pk(a0.z, a0.w));
        auto r0 = __builtin_amdgcn_permlane16_swap(o0, o0, false, false), r1 = __builtin_amdgcn_permlane16_swap(o1, o1, false, false);
        if (l < 16) ST128((u32x4{r0[0], r1[0], r0[1], r1[1]}), OFF_Z + ((4 * w + (l >> 2)) * 112 + mi) * 128 + (l & 3) * 16);
      }
    } else {
      x1s[col2] = x1; gbs[col2] = sb;
      BAR();
      if (w == 0) {
        const int f = 2 * g + (j - 30), col = 32 * f + 8 * (l & 3) + 4 * b4;
        f32x4 xv = *(const f32x4*)(x1s + col), gv = *(const f32x4*)(gbs + col);
        const u32 oZ = OFF_Z + (f * 112 + (l >> 2)) * 128 + (l & 3) * 16;
        u32x4 zz[7]; int n = 0; u32 mx;
        do { FENCE();
#pragma unroll
          for (int i = 0; i < 7; i++) zz[i] = LD128(oZ + i * 2048);
          mx = 0;
#pragma unroll
          for (int i = 0; i < 7; i++) mx = umax(mx, umax4(zz[i]));
        } while (any(mx == SENT) && ++n < SPIN);
        f32x4 a0 = z, a1 = z;
#pragma unroll
        for (int i = 0; i < 7; i++) { if (i & 1) a1 = mfma(ONE, zz[i], a1); else a0 = mfma(ONE, zz[i], a0); }
        a0 += a1;
#pragma unroll
        for (int i = 0; i < 4; i++) { a0[i] += dppf<ROR8>(a0[i]); a0[i] += dppf<ROR4>(a0[i]); a0[i] = xv[i] + rbf(gv[i] * rbf(a0[i])); }
        if ((l & 0x2C) == 0) *(u32x2*)(out + col) = u32x2{pk(a0.x, a0.y), pk(a0.z, a0.w)};
        if (f == 0 && l == 0) ST32(tag, OFF_EP);
        CNT(1, n);
      }
    }
  }
  asm volatile("" ::"v"(warm));
}

extern "C" void kernel_launch(void* const* d_in, const int* in_sizes, int n_in, void* d_out, int out_size, void* d_ws, size_t ws_size, hipStream_t stream) {
  fused_block<<<256, 512, 0, stream>>>((const u16*)d_in[0], (const u16*)d_in[1], (const u16*)d_in[2], (const u16*)d_in[3], (const u16*)d_in[4],
                                       (const u16*)d_in[6], (const u16*)d_in[7], (const u16*)d_in[8], (const u16*)d_in[9], (const u16*)d_in[10],
                                       (u16*)d_out, (char*)d_ws);
}
```

```cpp
#include <hip/hip_runtime.h>
#define WARM_KB 14
#define TC 120
#define PF_AT 4
#define TG 250
#define TG3 180
#define TM 220
#define TM3 150
#define W_SLEEP 24
#define D_SLEEP 4
typedef unsigned u32;
typedef unsigned short u16;
typedef unsigned long long u64;
typedef u32 u32x4 __attribute__((ext_vector_type(4)));
typedef u32 u32x2 __attribute__((ext_vector_type(2)));
typedef float f32x4 __attribute__((ext_vector_type(4)));
typedef float f32x2 __attribute__((ext_vector_type(2)));
typedef __bf16 bf16x8 __attribute__((ext_vector_type(8)));
typedef __bf16 bf16x2 __attribute__((ext_vector_type(2)));
typedef short s16x4 __attribute__((ext_vector_type(4)));
#define DEV __device__ __forceinline__
#define FENCE() do { asm volatile("" ::: "memory"); __builtin_amdgcn_sched_barrier(0); } while (0)
#define BAR() do { asm volatile("s_waitcnt lgkmcnt(0)" ::: "memory"); __builtin_amdgcn_s_barrier(); asm volatile("" ::: "memory"); } while (0)

#define STR2(x) #x
#define STR(x) STR2(x)

constexpr u32 OFF_EP = 0, OFF_H1 = 0x1000, OFF_T = 0x9000, OFF_H2 = 0x10000, OFF_M3 = 0x20000, OFF_Y = 0x24000, OFF_F4 = 0x26000,
              OFF_ML = 0x28000, OFF_P = 0x30000, OFF_Z = 0x50000, WS_BYTES = 0x60000;
constexpr u32 OOB = 0x40000000u;
constexpr int SPIN = 1 << 15;
constexpr u32 SENT = 0xFFFFFFFFu;

DEV float asf(u32 x) { return __builtin_bit_cast(float, x); }
DEV u32 asu(float x) { return __builtin_bit_cast(u32, x); }
DEV u32 pk(float a, float b) { bf16x2 v = __builtin_convertvector(f32x2{a, b}, bf16x2); return __builtin_bit_cast(u32, v); }
DEV float rbf(float a) { return asf(pk(0.f, a)); }
DEV float lo16(u32 x) { return asf(x << 16); }
DEV float hi16(u32 x) { return asf(x & 0xFFFF0000u); }
DEV float fmx(float a, float b) {
  return __builtin_fmaxf(a, b);
}
DEV u32 clampS(u32 x) { return x < 0xFFFFFFFEu ? x : 0xFFFFFFFEu; }
DEV u32 umax(u32 a, u32 b) { return a > b ? a : b; }
DEV u32 umax4(u32x4 v) { return umax(umax(v.x, v.y), umax(v.z, v.w)); }
template <int C> DEV u32 dppu(u32 x) { return __builtin_amdgcn_update_dpp(0, x, C, 0xF, 0xF, true); }
template <int C> DEV float dppf(float x) { return asf(dppu<C>(asu(x))); }
constexpr int ROR4 = 0x124, ROR8 = 0x128, HMIR = 0x141, QX1 = 0xB1, QX2 = 0x4E;
DEV float red32(float x, float y) { auto r = __builtin_amdgcn_permlane32_swap(asu(x), asu(y), false, false); return asf(r[0]) + asf(r[1]); }
DEV float red16(float x, float y) { auto r = __builtin_amdgcn_permlane16_swap(asu(x), asu(y), false, false); return asf(r[0]) + asf(r[1]); }
DEV float max32(float x) { auto r = __builtin_amdgcn_permlane32_swap(asu(x), asu(x), false, false); return fmx(asf(r[0]), asf(r[1])); }
DEV float max16(float x) { auto r = __builtin_amdgcn_permlane16_swap(asu(x), asu(x), false, false); return fmx(asf(r[0]), asf(r[1])); }
DEV float tred4(f32x4 a) { return red16(red32(a.x, a.z), red32(a.y, a.w)); }
DEV float sumhi(float x) { x += dppf<ROR4>(x); x += dppf<ROR8>(x); x = red16(x, x); return red32(x, x); }
DEV float maxhi(float x) { x = fmx(x, dppf<ROR4>(x)); x = fmx(x, dppf<ROR8>(x)); return max32(max16(x)); }
DEV float wsum(float x) { x += dppf<QX1>(x); x += dppf<QX2>(x); return sumhi(x); }
DEV f32x4 mfma(u32x4 a, u32x4 b, f32x4 c) {
  return __builtin_amdgcn_mfma_f32_16x16x32_bf16(__builtin_bit_cast(bf16x8, a), __builtin_bit_cast(bf16x8, b), c, 0, 0, 0);
}
struct Sel { u32 mv; f32x2 lo, hi; };
DEV u32x4 spread(u32 a, u32 b, const Sel& M) {
  f32x2 p = M.lo * asf(a & M.mv), q = M.hi * asf(b & M.mv);
  return u32x4{asu(p.x), asu(p.y), asu(q.x), asu(q.y)};
}
template <int I> DEV u32x4 selq(u32 x, const Sel& M) { u32 t = dppu<I * 0x55>(x); return spread(t, t, M); }
template <int S> DEV u32x4 sel8(u32 x, u32 xs, const Sel& M) {
  u32 a = S < 4 ? dppu<(S & 3) * 0x55>(x) : dppu<((7 - S) & 3) * 0x55>(xs);
  u32 b = S < 4 ? dppu<((3 - S) & 3) * 0x55>(xs) : dppu<(S & 3) * 0x55>(x);
  return spread(a, b, M);
}
#define LA(p) ((const __attribute__((address_space(3))) void*)(p))
#define RSRC(p) __builtin_amdgcn_make_buffer_rsrc((void*)(p), 0, 0x3FFFFFFF, 0x00020000)
#define BNT(r, v, s) __builtin_amdgcn_raw_buffer_load_b128(r, v, s, 2)
DEV float silu(float v) { return v * __builtin_amdgcn_rcpf(1.f + __expf(-v)); }
DEV u32x4 tagged(f32x4 v, u32 tag) {
  return u32x4{((asu(v.x) + 0x80u) & ~0xFFu) | (tag & 0xFF), ((asu(v.y) + 0x80u) & ~0xFFu) | ((tag >> 8) & 0xFF),
               ((asu(v.z) + 0x80u) & ~0xFFu) | ((tag >> 16) & 0xFF), ((asu(v.w) + 0x80u) & ~0xFFu) | (tag >> 24)};
}
DEV bool tagbad(u32x4 v, u32 tag) { return (__builtin_amdgcn_perm(v.y, v.x, 0x0c0c0400u) | __builtin_amdgcn_perm(v.w, v.z, 0x04000c0cu)) != tag; }
DEV f32x4 untag(u32x4 v) { return f32x4{asf(v.x & ~0xFFu), asf(v.y & ~0xFFu), asf(v.z & ~0xFFu), asf(v.w & ~0xFFu)}; }
DEV bool any(bool p) { return __builtin_amdgcn_ballot_w64(p) != 0; }
#define LD128(o) __builtin_amdgcn_raw_buffer_load_b128(rs, (o), 0, 16)
#define LD64(o) __builtin_amdgcn_raw_buffer_load_b64(rs, (o), 0, 16)
#define LD32(o) __builtin_amdgcn_raw_buffer_load_b32(rs, (o), 0, 16)
#define ST128(v, o) __builtin_amdgcn_raw_buffer_store_b128((v), rs, (o), 0, 16)
#define ST64(v, o) __builtin_amdgcn_raw_buffer_store_b64((v), rs, (o), 0, 16)
#define ST32(v, o) __builtin_amdgcn_raw_buffer_store_b32((v), rs, (o), 0, 16)
#define RETRY(bad, reload, ci) do { if (__builtin_expect(any(bad), 0)) { int n = 0; do { FENCE(); reload; ++n; } while (any(bad) && n < SPIN); } FENCE(); } while (0)

#define RAWBAR() do { FENCE(); __builtin_amdgcn_s_barrier(); FENCE(); } while (0)
#define SLEEP(n) do { if ((n) > 0) __builtin_amdgcn_s_sleep(n); FENCE(); } while (0)
#define WAITV(n) do { FENCE(); asm volatile("s_waitcnt vmcnt(" STR(n) ")" ::: "memory"); FENCE(); } while (0)
#define UN _Pragma("unroll")
template <int S> DEV void acc8(f32x4& a0, f32x4& a1, u32 x, u32 xs, const u32x4* W, const Sel& M) {
  a0 = mfma(sel8<S>(x, xs, M), W[S], a0); a1 = mfma(sel8<S + 1>(x, xs, M), W[S + 1], a1);
}
DEV f32x4 form8(u32 x, const u32x4* W, const Sel& M) {
  u32 xs = dppu<HMIR>(x);
  f32x4 a0 = {0, 0, 0, 0}, a1 = {0, 0, 0, 0};
  acc8<0>(a0, a1, x, xs, W, M); acc8<2>(a0, a1, x, xs, W, M); acc8<4>(a0, a1, x, xs, W, M); acc8<6>(a0, a1, x, xs, W, M);
  return a0 + a1;
}
template <int S> DEV void acc2(f32x4& a0, f32x4& a1, u32 x, u32 xs, const u32x4* W, const Sel& M) {
  u32x4 A = sel8<S>(x, xs, M); a0 = mfma(A, W[2 * S], a0); a1 = mfma(A, W[2 * S + 1], a1);
}
DEV void form8x2(u32 x, const u32x4* W, const Sel& M, f32x4& a0, f32x4& a1) {
  u32 xs = dppu<HMIR>(x);
  a0 = a1 = f32x4{0, 0, 0, 0};
  acc2<0>(a0, a1, x, xs, W, M); acc2<1>(a0, a1, x, xs, W, M); acc2<2>(a0, a1, x, xs, W, M); acc2<3>(a0, a1, x, xs, W, M);
  acc2<4>(a0, a1, x, xs, W, M); acc2<5>(a0, a1, x, xs, W, M); acc2<6>(a0, a1, x, xs, W, M); acc2<7>(a0, a1, x, xs, W, M);
}
DEV u32x4 pack8(f32x4 a) {
  u32 o0 = clampS(pk(a.x, a.y)), o1 = clampS(pk(a.z, a.w));
  auto r0 = __builtin_amdgcn_permlane16_swap(o0, o0, false, false), r1 = __builtin_amdgcn_permlane16_swap(o1, o1, false, false);
  return u32x4{r0[0], r1[0], r0[1], r1[1]};
}
DEV f32x4 ld4(const float* p) { return *(const f32x4*)p; }
#define POLL2(r, pa, pb, o, ci) do { int k = 0; \
    for (;;) { FENCE(); pb = LD128(o); FENCE(); if (!any(umax4(pa) == SENT)) { r = pa; break; } \
               FENCE(); pa = LD128(o); FENCE(); if (!any(umax4(pb) == SENT) || ++k >= SPIN) { r = pb; break; } } } while (0)
constexpr float LOG2E = 1.4426950408889634f;

__global__ void __launch_bounds__(512, 2)
fused_block(const u16* x, const u16* Kc, const u16* Vc, const u16* wq, const u16* wo, const u16* wg, const u16* wu, const u16* wd,
            const u16* c, const u16* wm, u16* out, char* ws) {
  __shared__ __attribute__((aligned(16))) char kv[256 * 288];
  __shared__ __attribute__((aligned(16))) u16 qs[4 * 136];
  __shared__ __attribute__((aligned(16))) u32 Pl[4 * 132];
  __shared__ __attribute__((aligned(16))) float mw[512];
  u32 warm = 0;
  { u64 pc; asm volatile("s_getpc_b64 %0" : "=s"(pc));
    u32 t = threadIdx.x >> 2;
    warm = *(const __attribute__((address_space(1))) u32*)((pc & ~127ull) + (u64)(u32)((t < WARM_KB * 8 - 1 ? t : WARM_KB * 8 - 1) * 128)); }
  FENCE();
  asm volatile("" ::"s"(x), "s"(wd), "s"(c), "s"(ws));
  const int b = blockIdx.x; int g = b & 7, j = b >> 3;
  const int l = threadIdx.x & 63, w = __builtin_amdgcn_readfirstlane(threadIdx.x >> 6);
  const int kg = l >> 4, n = l & 15, b5 = l >> 5, b4 = kg & 1, b3 = (l >> 3) & 1, b2 = (l >> 2) & 1;
  const auto rs = __builtin_amdgcn_make_buffer_rsrc(ws, 0, WS_BYTES, 0x00020000);
  const u32 ep = *(const __attribute__((address_space(4))) u32*)(u64)ws;
#define MKTAG(t) u32 t; asm volatile("s_add_u32 %0, %1, 0x01010101" : "=s"(t) : "s"(ep) : "scc")
  const f32x4 z = {0, 0, 0, 0};
  const Sel M = {(l & 1) ? 0xFFFF0000u : 0xFFFFu, f32x2{(l & 6) == 0 ? 1.f : 0.f, (l & 6) == 2 ? 1.f : 0.f}, f32x2{(l & 6) == 4 ? 1.f : 0.f, (l & 6) == 6 ? 1.f : 0.f}};
  if (__builtin_expect(j < 16 && w < 2 && g < 4, 1)) {
    u32x4 WA[8];
    const int gp = 2 * g + (j >> 3), ca = 2 * (j & 7) + w;
    u32 cA = __builtin_amdgcn_raw_buffer_load_b16(RSRC(c), 16 * (l & 7) + 2 * (l >> 3), 128 * gp, 0);
    UN for (int i = 0; i < 8; i++) WA[i] = BNT(RSRC(wm), (l >> 3) * 6144 + 16 * (l & 7), (64 * gp + 8 * i) * 6144 + 128 * ca);
    FENCE();
    MKTAG(tA);
    const u32 now = (u32)__builtin_amdgcn_s_memrealtime();
    FENCE();
    float s = silu(lo16(cA));
    f32x4 a = form8(pk(s, s), WA, M);
    UN for (int i = 0; i < 4; i++) {
      auto sw = __builtin_amdgcn_permlane32_swap(asu(a[i]), asu(a[i]), false, false);
      a[i] += dppf<ROR8>(asf(sw[1]));
    }
    u32x4 v = tagged(a, tA);
    if (l == 8) { v.x = tA; v.y = now; }
    ST128(v, (l & 0x28) == 0 ? OFF_H1 + ((2 * ca + b2) * 8 + gp) * 128 + b4 * 64 + (l & 3) * 16 : l + w == 8 ? OFF_T + (g * 16 + j) * 128 : OOB);
  }
  asm volatile("" : "+s"(j), "+s"(g));
  const bool early = g < 4;
  if (__builtin_expect_with_probability(j < 16, 1, 0.999)) {
    const u32 kvo = (g * 32768 + 28672 + 256 * j + 32 * w) * 256;
    u32x4 Vr[8], Kr[8], Vf[8], Kf[8];
    if (early) RAWBAR();
    FENCE();
    UN for (int i = 0; i < 8; i++) Vr[i] = BNT(RSRC(Vc), 16 * l + 1024 * (i & 3), kvo + 4096 * (i >> 2));
    RAWBAR();
    UN for (int i = 0; i < 8; i++) Kr[i] = BNT(RSRC(Kc), 16 * l + 1024 * (i & 3), kvo + 4096 * (i >> 2));
    FENCE();
    MKTAG(tag);
    char* kvw = kv + (32 * w + kg) * 288 + n * 16;
    UN for (int i = 0; i < 8; i++) *(u32x4*)(kvw + 4 * i * 288) = Vr[i];
    BAR();
    const u32 oH2 = OFF_H2 + g * 8192 + (w >> 1) * 2048 + (w & 1) * 256 + kg * 512 + n * 16;
    u32x4 qv;
    qv = LD128(oH2);
    FENCE();
    UN for (int i = 0; i < 8; i++) {
      typedef __attribute__((address_space(3))) s16x4* lp;
      char* p = kv + (32 * i + 4 * kg + b3 + 16 * b2) * 288 + 32 * w + 8 * (l & 3);
      u32x2 t0 = __builtin_bit_cast(u32x2, __builtin_amdgcn_ds_read_tr16_b64_v4i16((lp)p));
      u32x2 t1 = __builtin_bit_cast(u32x2, __builtin_amdgcn_ds_read_tr16_b64_v4i16((lp)(p + 2 * 288)));
      Vf[i] = u32x4{t0.x, t0.y, t1.x, t1.y};
    }
    BAR();
    UN for (int i = 0; i < 4; i++) *(u32x4*)(kvw + 4 * i * 288) = Kr[i];
    UN for (int i = 0; i < 4; i++) Kf[i] = *(const u32x4*)(kv + (32 * w + n) * 288 + (4 * i + kg) * 16);
    FENCE();
    UN for (int i = 4; i < 8; i++) *(u32x4*)(kvw + 4 * i * 288) = Kr[i];
    UN for (int i = 0; i < 4; i++) Kf[4 + i] = *(const u32x4*)(kv + (32 * w + 16 + n) * 288 + (4 * i + kg) * 16);
    const u32 oP = (l & 7) ? OOB : OFF_P + ((g * 8 + w) * 16 + j) * 128 + l * 2;
    const u32 oM = n == 0 ? OFF_M3 + (g * 8 + (j & 7)) * 128 + (j >> 3) * 8 + kg * 16 : OOB;
    asm volatile("" ::"v"(oP), "v"(oM));
    FENCE();
    RETRY(tagbad(qv, tag), qv = LD128(oH2), 13);
    { float q = tred4(untag(qv)); qs[(w >> 1) * 136 + (w & 1) * 64 + 4 * n + kg] = (u16)pk(q, q); }
    BAR();
    {
      f32x4 a0 = z, a1 = z;
      UN for (int i = 0; i < 4; i++) {
        u32x4 q = *(const u32x4*)(qs + (n >> 2) * 136 + 32 * i + 8 * kg);
        a0 = mfma(q, Kf[i], a0); a1 = mfma(q, Kf[4 + i], a1);
      }
      u32 s = pk(a0.x, a1.x); s = pk(lo16(s) * 0.08838834764831845f, hi16(s) * 0.08838834764831845f);
      float s0 = lo16(s), s1 = hi16(s), m = fmx(s0, s1);
      m = fmx(m, dppf<QX1>(m)); m = fmx(m, dppf<QX2>(m)); m = fmx(m, dppf<ROR4>(m)); m = fmx(m, dppf<ROR8>(m));
      mw[64 * w + l] = m;
      Pl[kg * 132 + 16 * w + n] = pk(__builtin_amdgcn_exp2f((s0 - m) * LOG2E), __builtin_amdgcn_exp2f((s1 - m) * LOG2E));
    }
    BAR();
    {
      float m = mw[64 * (l & 7) + l];
      const u32* p = Pl + (n >> 2) * 132 + 16 * (l & 3) + 4 * kg;
      u32x4 P0 = *(const u32x4*)p, P1 = *(const u32x4*)(p + 64);
      float Mx = fmx(m, dppf<QX1>(m)); Mx = fmx(Mx, dppf<QX2>(Mx)); Mx = fmx(Mx, dppf<ROR4>(Mx));
      float f = __builtin_amdgcn_exp2f((m - Mx) * LOG2E), fo = dppf<ROR4>(f), f0 = b2 ? fo : f, f1 = b2 ? f : fo;
      f32x4 d0, d1;
      UN for (int i = 0; i < 4; i++) { d0[i] = mfma(P0, Vf[i], z)[i]; d1[i] = mfma(P1, Vf[4 + i], z)[i]; }
#define WSUM8(a, b) (dppf<0x00>(f0) * a.x + dppf<0x55>(f0) * a.y + dppf<0xAA>(f0) * a.z + dppf<0xFF>(f0) * a.w) + \
                    (dppf<0x00>(f1) * b.x + dppf<0x55>(f1) * b.y + dppf<0xAA>(f1) * b.z + dppf<0xFF>(f1) * b.w)
      float o = WSUM8(d0, d1);
      u32 o2 = clampS(pk(o, dppf<QX1>(o)));
      ST128((u32x4{o2, dppu<0x102>(o2), dppu<0x104>(o2), dppu<0x106>(o2)}), oP);
      if (w == 0) {
        const u32x4 one = {0x3F803F80u, 0x3F803F80u, 0x3F803F80u, 0x3F803F80u};
        d0 = mfma(P0, one, z); d1 = mfma(P1, one, z);
        float ls = WSUM8(d0, d1);
        ST64((u32x2{clampS(asu(Mx)), clampS(asu(ls))}), oM);
      }
    }
  } else {
    float* red = (float*)kv; float* mods = red + 4096;
    constexpr int R2 = 2080;
    const bool qb = early && j >= 16;
    const int slot = j - 16, mi = 4 * (slot >> 1) + (g & 3);
    const int u = j & 7;
    g = qb ? 2 * g + ((j >> 3) & 1) : mi & 7;
    const int colx = 8 * (l & 7) + 4 * b4 + 2 * b5 + b3;
    float* rp = red + (2 * w + b3) * 80 + 8 * (l & 7) + 4 * b4;
    u32x4 L2[8], L3[16];
    u16 xc;
    if (qb) {
      const int ks = u & 3, row0 = 128 * ks + 32 * (w >> 1) + 4 * (w & 1);
      FENCE();
      u32x4 xf = __builtin_amdgcn_raw_buffer_load_b128(RSRC(x), 16 * l, 0, 0);
      u32 xr = __builtin_amdgcn_raw_buffer_load_b16(RSRC(x), 2 * kg + 16 * (l & 3), 2 * row0, 0);
      FENCE();
      UN for (int i = 0; i < 8; i++) L2[i] = BNT(RSRC(wq), kg * 8192 + 16 * n + 256 * (i & 1), (row0 + 8 * (i >> 1)) * 8192 + 1024 * g + 512 * (u >> 2));
      FENCE();
      {
        u32 o = OFF_P + ((g * 8 + (l >> 3)) * 16 + 2 * u + w) * 128 + (l & 7) * 16;
        if (w == 2) o = l < 8 ? OFF_Y + (u * 8 + g) * 128 + l * 16 : l < 12 ? OFF_M3 + (g * 8 + u) * 128 + (l & 3) * 16 : OOB;
        if (w == 3) o = u < 7 ? OFF_Z + g * 7168 + u * 1024 + l * 16 : OOB;
        ST128((u32x4{SENT, SENT, SENT, SENT}), w < 4 ? o : OOB);
      }
      FENCE();
      MKTAG(tag);
      const u32 oH1 = OFF_H1 + ((b2 * 16 + 4 * ks + (w >> 1)) * 8 + (l >> 3)) * 128 + (w & 1) * 64 + (l & 3) * 16;
      RAWBAR();
      FENCE();
      u32x4 hv = LD128(oH1);
      FENCE();
      float ln;
      { float v[8] = {lo16(xf.x), hi16(xf.x), lo16(xf.y), hi16(xf.y), lo16(xf.z), hi16(xf.z), lo16(xf.w), hi16(xf.w)};
        float s = 0;
        UN for (int i = 0; i < 8; i++) s += v[i];
        float mean = wsum(s) * (1.f / 512), q = 0;
        UN for (int i = 0; i < 8; i++) q += (v[i] - mean) * (v[i] - mean);
        float var = wsum(q) * (1.f / 512);
        float sd = rbf(sqrtf(rbf(rbf(var) + 1e-5f)));
        ln = rbf(rbf(lo16(xr) - rbf(mean)) / sd);
      }
      float* pw = red + 256 * w + 128 * b5 + 8 * n + 4 * b4;
      const float* pq = red + 1024 * b5 + 128 * w + 4 * (l & 31);
      const u32 oQ = b5 ? OOB : OFF_H2 + g * 8192 + (u >> 2) * 4096 + w * 2048 + ks * 512 + l * 16;
      asm volatile("" ::"v"(ln), "v"(LA(pw)), "v"(LA(pq)), "v"(oQ));
      FENCE();
      RETRY(tagbad(hv, tag), hv = LD128(oH1), 12);
      { float v = tred4(untag(hv)); v += dppf<ROR8>(v); v = rbf(v);
        float o = dppf<ROR4>(v), sa = b2 ? o : v, ca = b2 ? v : o;
        float h = rbf(ln * rbf(1.f + ca)) + sa;
        u32 hd = pk(h, h);
        f32x4 a0 = z, a1 = z;
#define QSTEP(i) { u32x4 A = selq<i>(hd, M); a0 = mfma(A, L2[2 * i], a0); a1 = mfma(A, L2[2 * i + 1], a1); }
        QSTEP(0) QSTEP(1) QSTEP(2) QSTEP(3)
        *(f32x4*)pw = b5 ? a1 : a0;
      }
      BAR();
      if (w < 2) {
        const float* p = pq;
        f32x4 a = (ld4(p) + ld4(p + 256)) + (ld4(p + 512) + ld4(p + 768));
        UN for (int i = 0; i < 4; i++) a[i] = red32(a[i], a[i]);
        ST128(tagged(a, tag), oQ);
      }
      RAWBAR();
#define PROOF() do { int k = 0; u32x4 f; do { FENCE(); f = LD128(OFF_H2 + g * 8192 + n * 512); } while (any(tagbad(f, tag)) && ++k < SPIN); } while (0)
      const int cls = (((u32)(u64)wo >> 7) + u) & 7;
      if (cls != 3) {
        if (w == 0) { PROOF(); if (cls == 0) ST32(tag, l ? OOB : OFF_F4 + g * 128); FENCE(); }
        else SLEEP(W_SLEEP);
      }
      const bool lp = cls == 3 && w == 0;
      u32x4 pf;
      UN for (int i = 0; i < 8; i++) {
        if (i == PF_AT) { FENCE(); pf = LD128(lp ? OFF_H2 + g * 8192 + n * 512 : OOB); FENCE(); }
        L2[i] = BNT(RSRC(wo), (l >> 3) * 1024 + 16 * (l & 7), (512 * g + 128 * (i >> 1) + 16 * w + 8 * (i & 1)) * 1024 + 128 * u);
      }
      FENCE();
      const u32 oP = OFF_P + ((g * 8 + w) * 16) * 128 + l * 16, oM = OFF_M3 + (g * 8 + (l >> 3)) * 128 + (l & 6) * 8;
      if (lp) { if (any(tagbad(pf, tag))) PROOF(); }
      const float* py = red + ((l >> 2) & 3) * 80 + 4 * (8 * b5 + 2 * (l & 3) + b4);
      const u32 oY = (l & 0x1C) ? OOB : OFF_Y + (u * 8 + g) * 128 + b5 * 64 + (l & 3) * 16;
      asm volatile("" ::"v"(LA(py)), "v"(oY), "v"(LA(rp)));
      RAWBAR();
      FENCE();
      u32x4 p0 = LD128(oP), p1 = LD128(oP + 1024), ml = LD128(oM);
      FENCE();
      { int k = 0;
        for (;;) {
          bool bad = umax(umax(umax4(p0), umax4(p1)), umax4(ml)) == SENT;
          if (!any(bad) || ++k >= SPIN) break;
          FENCE();
          if (bad) { p0 = LD128(oP); p1 = LD128(oP + 1024); ml = LD128(oM); }
        }
 }
      {
        float mx = fmx(asf(ml.x), asf(ml.z)); mx = max32(max16(fmx(mx, dppf<ROR8>(mx))));
        float e0 = __builtin_amdgcn_exp2f((asf(ml.x) - mx) * LOG2E), e1 = __builtin_amdgcn_exp2f((asf(ml.z) - mx) * LOG2E);
        float ls = e0 * asf(ml.y) + e1 * asf(ml.w); ls += dppf<ROR8>(ls); ls = red16(ls, ls); ls = red32(ls, ls);
        float a[8];
        UN for (int i = 0; i < 4; i++) {
          f32x2 t = f32x2{lo16(p0[i]), hi16(p0[i])} * e0 + f32x2{lo16(p1[i]), hi16(p1[i])} * e1;
          a[2 * i] = t.x; a[2 * i + 1] = t.y;
        }
        float t0 = red16(red32(a[0], a[4]), red32(a[2], a[6])), t1 = red16(red32(a[1], a[5]), red32(a[3], a[7]));
        float o = ((b3 ? t1 : t0) + dppf<ROR8>(b3 ? t0 : t1)) * __builtin_amdgcn_rcpf(ls);
        f32x4 r0 = form8(pk(o, o), L2, M);
        if (b5 == b3) *(f32x4*)rp = r0;
      }
      BAR();
      if (w == 0) {
        const float* p = py;
        f32x4 a = (ld4(p) + ld4(p + 320)) + (ld4(p + 640) + ld4(p + 960));
        UN for (int i = 0; i < 4; i++) { a[i] += dppf<ROR8>(a[i]); a[i] += dppf<ROR4>(a[i]); }
        ST128(pack8(a), oY);
      }
    }
    int qo = b; asm volatile("" : "+s"(qo));
    if ((qo & 0x84) != 0x80) {
      const bool mlp = slot & 1;
      const int cw = mlp ? w : g;
      xc = __builtin_amdgcn_raw_buffer_load_b16(RSRC(x), 2 * colx, 128 * cw, 0);
      FENCE();
      MKTAG(tag);
      if (w == 7) {
        int k = 0; u32x4 f;
        do { FENCE(); f = LD128(OFF_T + ((g & 3) * 16 + slot) * 128); } while (any(f.x != tag) && ++k < SPIN);
        const u32 t0 = k ? (u32)__builtin_amdgcn_s_memrealtime() - TC : __builtin_amdgcn_readfirstlane(f.y);
        const u32 dt = mlp ? (((((u32)(u64)wg >> 7) + mi) & 3) != 3 ? TG : TG3) : (((((u32)(u64)wm >> 7) + mi) & 7) != 3 ? TM : TM3);
        for (k = 0; (u32)__builtin_amdgcn_s_memrealtime() - t0 < dt && k < 100; k++) __builtin_amdgcn_s_sleep(1);
      }
      RAWBAR();
#define LOADWD(aux, vo) UN for (int i = 0; i < 8; i++) L2[i] = __builtin_amdgcn_raw_buffer_load_b128(RSRC(wd), vo, (64 * mi + 32 * (w >> 2) + 4 * i) * 1024 + 256 * (w & 3), aux)
      if (!mlp) {
        u32 cM = __builtin_amdgcn_raw_buffer_load_b16(RSRC(c), 16 * (l & 7) + 2 * (l >> 3), 128 * w, 0);
        UN for (int i = 0; i < 8; i++) L2[i] = BNT(RSRC(wm), (l >> 3) * 6144 + 16 * (l & 7), (64 * w + 8 * i) * 6144 + 2048 + 128 * mi);
        FENCE();
        { float s = silu(lo16(cM));
          f32x4 a = form8(pk(s, s), L2, M);
          if (b5 == b3) *(f32x4*)rp = a; }
        BAR();
        if (w == 0) {
          const float* p = red + 320 * kg + 4 * n;
          f32x4 a = (ld4(p) + ld4(p + 80)) + (ld4(p + 160) + ld4(p + 240));
          UN for (int i = 0; i < 4; i++) { a[i] = red16(a[i], a[i]); a[i] = red32(a[i], a[i]); }
          ST128(tagged(a, tag), kg ? OOB : OFF_ML + mi * 256 + l * 16);
        }
        if (mi < 28) {
          RAWBAR();
          LOADWD(0, kg * 1024 + 16 * n);
          UN for (int i = 0; i < 8; i++) asm volatile("" ::"v"(L2[i]));
        }
        if (mi >= 8) goto done;
      }
      int mo = slot; asm volatile("" : "+s"(mo));
      if (mo & 1) {
        if (mi >= 28) goto done;
        UN for (int i = 0; i < 16; i++) L3[i] = BNT(RSRC((i & 1) ? wu : wg), (4 * b4 + 2 * b5 + b3) * 3584 + 16 * (l & 7), (64 * w + 8 * (i >> 1)) * 3584 + 128 * mi);
        RAWBAR();
      }
      float ga, sb, cb;
      { const u32 o = OFF_ML + (kg * 8 + cw) * 256 + n * 16;
        const u32 of = OFF_F4 + (l & 7) * 128;
        u32x4 m0 = LD128(o); u32 f = LD32(of); int k = 0;
        RAWBAR(); LOADWD(2, mlp ? kg * 1024 + 16 * n : 0x70000000);
        FENCE();
        if (__builtin_expect(any(tagbad(m0, tag) || f != tag), 0))
          do { __builtin_amdgcn_s_sleep(2); FENCE(); m0 = LD128(o); f = LD32(of); } while (any(tagbad(m0, tag) || f != tag) && ++k < SPIN);
        f32x4 a = untag(m0);
        float* p = mods + 256 * w;
        *(f32x4*)(p + 4 * l) = a;
        FENCE();
        ga = rbf(p[colx]); cb = rbf(1.f + rbf(p[128 + colx])); sb = rbf(p[(mlp ? 64 : 192) + colx]);
      }
      if (mlp) {
        f32x4 r0, r1;
        form8x2(pk(cb, cb), L3, M, r0, r1);
        if (b5 == b3) { *(f32x4*)(rp + 10240) = r0; *(f32x4*)(rp + 10240 + R2) = r1; }
        form8x2(pk(sb, sb), L3, M, r0, r1);
        if (b5 == b3) { *(f32x4*)(rp + 6144) = r0; *(f32x4*)(rp + 6144 + R2) = r1; }
        float s = sumhi(lo16(xc)); s += dppf<QX1>(s); s += dppf<QX2>(s);
        if (l == 0) mw[64 + w] = s;
      }
      FENCE();
      const u32 oY = OFF_Y + cw * 1024 + l * 16;
      const u32x4 ONE = spread(0x3F803F80u, 0x3F803F80u, M);
      const float* pg = red + R2 * b3 + 320 * kg + 32 * (w >> 2) + 4 * (l & 7);
      const u32 oZ = mlp ? (kg ? OOB : OFF_Z + ((2 * (w & 3) + b3) * 56 + 2 * mi + (w >> 2)) * 128 + (l & 7) * 16) : OFF_Z + g * 7168 + w * 1024 + l * 16;
      asm volatile("" ::"v"(ONE), "v"(LA(pg)), "v"(oZ), "v"(LA(rp)), "v"(ga), "v"(sb), "v"(cb));
      u32x4 y0, ya, yb;
      float cw_ = 0, sw_ = 0, m0 = 0;
      ya = LD128(oY); FENCE();
      if (mlp) {
        BAR();
        cw_ = tred4((ld4(pg + 10240) + ld4(pg + 10320)) + (ld4(pg + 10400) + ld4(pg + 10480)));
        sw_ = tred4((ld4(pg + 6144) + ld4(pg + 6224)) + (ld4(pg + 6304) + ld4(pg + 6384)));
        f32x4 a = ld4(mw + 64) + ld4(mw + 68);
        m0 = ((a.x + a.y) + (a.z + a.w)) * (1.f / 512);
      }
      asm volatile("" ::"v"(cw_), "v"(sw_), "v"(m0));
      POLL2(y0, ya, yb, oY, 15);
      float x1;
      { f32x4 a = mfma(ONE, y0, z);
        float v0 = b5 ? a.z : a.x, v1 = b5 ? a.w : a.y;
        float y = (b3 ? v1 : v0) + dppf<ROR8>(b3 ? v0 : v1);
        x1 = rbf(lo16(xc) + rbf(ga * rbf(y))); }
      if (mlp) {
        const float d = x1 - m0, ud = d * cb;
        f32x4 r0, r1;
        form8x2(pk(ud, ud), L3, M, r0, r1);
        float s = red32(d, d * d); s += dppf<QX1>(s); s += dppf<QX2>(s); s += dppf<ROR4>(s); s += dppf<ROR8>(s); s = red16(s, s);
        if ((l & 31) == 0) mw[2 * w + b5] = s;
        if (b5 == b3) { *(f32x4*)rp = r0; *(f32x4*)(rp + R2) = r1; }
        BAR();
        {
          f32x2 sv = *(const f32x2*)(mw + 2 * (l & 7));
          float v = tred4((ld4(pg) + ld4(pg + 80)) + (ld4(pg + 160) + ld4(pg + 240)));
          float mean = sv.x, t = sv.y;
          mean += dppf<QX1>(mean); t += dppf<QX1>(t); mean += dppf<QX2>(mean); t += dppf<QX2>(t); mean += dppf<HMIR>(mean); t += dppf<HMIR>(t);
          mean *= 1.f / 512;
          float sd = rbf(__builtin_amdgcn_sqrtf(rbf(rbf(fmx(t * (1.f / 512) - mean * mean, 0.f)) + 1e-5f)));
          v = rbf((v - mean * cw_) * __builtin_amdgcn_rcpf(sd) + sw_);
          v *= dppf<ROR8>(v);
          ST128(pack8(form8(pk(v, v), L2, M)), oZ);
        }
      } else {
        FENCE();
        if (w < 7) {
          u32x4 z0, za, zb;
          za = LD128(oZ); SLEEP(D_SLEEP);
          POLL2(z0, za, zb, oZ, 13);
          f32x4 a = mfma(ONE, z0, z);
          float v0 = b5 ? a.z : a.x, v1 = b5 ? a.w : a.y;
          mw[64 * w + l] = (b3 ? v1 : v0) + dppf<ROR8>(b3 ? v0 : v1);
          BAR();
          asm volatile("" ::"v"(za), "v"(zb));
        } else {
          u16* po = out + 64 * g + colx;
          const float* p = mw + l;
          asm volatile("" ::"v"(po), "v"(LA(p)));
          BAR();
          float y = ((p[0] + p[64]) + (p[128] + p[192])) + ((p[256] + p[320]) + p[384]);
          y = x1 + rbf(sb * rbf(y));
          *po = (u16)pk(y, y);
          if (g == 0) ST32(tag, l ? OOB : OFF_EP);
        }
      }
      asm volatile("" ::"v"(ya), "v"(yb));
    }
  }
done:
  asm volatile("" ::"v"(warm));
}

extern "C" void kernel_launch(void* const* d_in, const int* in_sizes, int n_in, void* d_out, int out_size, void* d_ws, size_t ws_size, hipStream_t stream) {
  fused_block<<<256, 512, 0, stream>>>((const u16*)d_in[0], (const u16*)d_in[1], (const u16*)d_in[2], (const u16*)d_in[3], (const u16*)d_in[4],
                                       (const u16*)d_in[6], (const u16*)d_in[7], (const u16*)d_in[8], (const u16*)d_in[9], (const u16*)d_in[10],
                                       (u16*)d_out, (char*)d_ws);
}
```
